```python
import torch

def _softmax(x, axis=-1):
    return torch.softmax(x, dim=axis)

def _gelu(x):
    return torch.nn.functional.gelu(x, approximate='tanh')

def _silu(x):
    return torch.nn.functional.silu(x)

def _sigmoid(x):
    return torch.sigmoid(x)

def _layernorm(x, axis=-1, eps=1e-05):
    m = x.mean(dim=axis, keepdim=True)
    v = x.var(dim=axis, keepdim=True, unbiased=False)
    return (x - m) / torch.sqrt(v + eps)

def _scatter_add(tgt, idx, src, axis=0):
    return tgt.clone().index_add_(axis, idx, src)

def _segment_sum(src, seg, n):
    out = torch.zeros((n,) + src.shape[1:], dtype=src.dtype)
    return out.index_add_(0, seg, src)

def _pad(x, pw):
    flat = tuple((v for pair in reversed(pw) for v in pair))
    return torch.nn.functional.pad(x, flat)
kernel_name = 'standalone__tree_speculative_verify_tree_attention_over_candidate_token_tree__decode'

def setup_inputs():

    _w = lambda a: (lambda t: t.to(torch.bfloat16) if t.is_floating_point() else t)(torch.as_tensor(a))
    B, Hq, Hkv, T, C, D = (2, 32, 32, 1, 8192, 192)
    tree_mask = torch.zeros((B, T, T), dtype=float)
    for b in range(B):
        parent = torch.zeros(T, dtype=torch.int64)
        for i in range(1, T):
            parent[i] = int(torch.randint(0, i, (1,)).item())
        for i in range(T):
            j = i
            tree_mask[b, i, j] = 1.0
            while j > 0:
                j = parent[j]
                tree_mask[b, i, j] = 1.0
    return {'q': _w((lambda *s: torch.randn(*s, dtype=torch.bfloat16) * 0.8)(B, Hq, T, D)), 'k_cache': _w((lambda *s: torch.randn(*s, dtype=torch.bfloat16) * 0.8)(B, Hkv, C, D)), 'v_cache': _w((lambda *s: torch.randn(*s, dtype=torch.bfloat16) * 0.05)(B, Hkv, C, D)), 'k_tree': _w((lambda *s: torch.randn(*s, dtype=torch.bfloat16) * 0.8)(B, Hkv, T, D)), 'v_tree': _w((lambda *s: torch.randn(*s, dtype=torch.bfloat16) * 0.05)(B, Hkv, T, D)), 'tree_mask': _w(tree_mask)}

def reference(q, k_cache, v_cache, k_tree, v_tree, tree_mask):
    B, Hq, T, D = q.shape
    Hkv, C = (k_cache.shape[1], k_cache.shape[2])
    rep = Hq // Hkv
    kc = (lambda x, n, axis: torch.repeat_interleave(x, n, dim=axis))(k_cache, rep, axis=1)
    vc = (lambda x, n, axis: torch.repeat_interleave(x, n, dim=axis))(v_cache, rep, axis=1)
    kt = (lambda x, n, axis: torch.repeat_interleave(x, n, dim=axis))(k_tree, rep, axis=1)
    vt = (lambda x, n, axis: torch.repeat_interleave(x, n, dim=axis))(v_tree, rep, axis=1)
    k = torch.concatenate([kc, kt], axis=2)
    v = torch.concatenate([vc, vt], axis=2)
    scale = D ** (-0.5)
    scores = torch.einsum('bhqd,bhkd->bhqk', q, k) * scale
    ctx_mask = torch.ones((B, 1, T, C), dtype=scores.dtype)
    tm = tree_mask[:, None, :, :].clone()
    full_mask = torch.concatenate([ctx_mask, tm], axis=-1)
    scores = scores + (1.0 - full_mask) * -1000000000.0
    p = _softmax(scores, axis=-1)
    return torch.einsum('bhqk,bhkd->bhqd', p, v)
```

```cpp
#define PR 5
#include <hip/hip_runtime.h>
#include <hip/hip_bf16.h>
#include <stdint.h>

#define G1N 6
#define G1S 3
#define G2N 7
#define G2S 2
#define MIX 5
#define PH3 8
#define CODEPF 5760
#define SH1 2
#define SH2 5
#define DK 15
#define DV 15
#define NOPS(N) do { if ((N) >= 0) { asm volatile("s_nop %0" :: "i"((N) & 15)); if ((N) >= 16) asm volatile("s_nop 15"); } } while (0)
#define BACKOFF 0
#define WAVES 1
#define UNR 2

constexpr int NB = 2, NH = 32, BH = NB * NH, CTX = 8192, D = 192;
constexpr int NW = 8;
constexpr int ROWB = D * 2;
constexpr int TB = 8 * UNR * ROWB;
constexpr int NT = CTX / (8 * UNR);
constexpr int KVB = BH * CTX * ROWB;
constexpr int NS0 = 63 - PR;
constexpr int POOL = PR * NW;
constexpr int G3N = POOL - G1N * G1S - G2N * G2S;
static_assert(SH1 * 7 < 42 && SH2 * 7 < 42 && PR % 2 == 1 && PR <= 17 && G3N >= 0 && G1N + G2N + G3N <= 62, "ticket plan");
constexpr int NREC = 3 * NW + 1;
constexpr int RECB = 512;
constexpr int WS_EPOCH = 0;
constexpr int WS_TICKET = 4096, TKB = 256;
constexpr int WS_REC = WS_TICKET + BH * TKB;
constexpr int AUX_NT = 2, AUX_SC1 = 16;
constexpr int OOB = 0x3fff0000;
constexpr unsigned TAGSTEP = 0x5bd1e995u;

using bf16x2 = __attribute__((ext_vector_type(2))) __bf16;
using bf16x8 = __attribute__((ext_vector_type(8))) __bf16;
using u32x2 = __attribute__((ext_vector_type(2))) unsigned;
using u32x4 = __attribute__((ext_vector_type(4))) unsigned;
using f32x4 = __attribute__((ext_vector_type(4))) float;
using f32x2 = __attribute__((ext_vector_type(2))) float;
typedef unsigned long long u64;

using i32x4 = __attribute__((ext_vector_type(4))) int;
__device__ u32x4 buf_ld128(i32x4, int, int, int) __asm("llvm.amdgcn.raw.buffer.load.v4i32");
__device__ long long buf_add64(long long, __amdgpu_buffer_rsrc_t, int, int, int) __asm("llvm.amdgcn.raw.ptr.buffer.atomic.add.i64");

__device__ __forceinline__ float dot2(unsigned a, unsigned b, float c) {
  return __builtin_amdgcn_fdot2_f32_bf16(__builtin_bit_cast(bf16x2, a), __builtin_bit_cast(bf16x2, b), c, false);
}
template <int CTRL> __device__ __forceinline__ float dpp(float x) {
  return __builtin_bit_cast(float, __builtin_amdgcn_mov_dpp(__builtin_bit_cast(int, x), CTRL, 0xf, 0xf, true));
}
constexpr int XOR1 = 0xB1, XOR2 = 0x4E, XOR7 = 0x141, XOR8 = 0x128;
__device__ __forceinline__ float x16_sum(float x) {
  auto s = __builtin_amdgcn_permlane16_swap(__float_as_uint(x), __float_as_uint(x), false, false);
  return __uint_as_float(s[0]) + __uint_as_float(s[1]);
}
__device__ __forceinline__ float x32_sum(float x) {
  auto s = __builtin_amdgcn_permlane32_swap(__float_as_uint(x), __float_as_uint(x), false, false);
  return __uint_as_float(s[0]) + __uint_as_float(s[1]);
}
__device__ __forceinline__ float x16_max(float x) {
  auto s = __builtin_amdgcn_permlane16_swap(__float_as_uint(x), __float_as_uint(x), false, false);
  return fmaxf(__uint_as_float(s[0]), __uint_as_float(s[1]));
}
__device__ __forceinline__ float x32_max(float x) {
  auto s = __builtin_amdgcn_permlane32_swap(__float_as_uint(x), __float_as_uint(x), false, false);
  return fmaxf(__uint_as_float(s[0]), __uint_as_float(s[1]));
}
__device__ __forceinline__ unsigned pk_bf16(float lo, float hi) {
  f32x2 v = {lo, hi};
  return __builtin_bit_cast(unsigned, __builtin_convertvector(v, bf16x2));
}
__device__ __forceinline__ f32x4 mfma(u32x4 a, u32x4 b, f32x4 c) {
  return __builtin_amdgcn_mfma_f32_16x16x32_bf16(__builtin_bit_cast(bf16x8, a), __builtin_bit_cast(bf16x8, b), c, 0, 0, 0);
}
__device__ __forceinline__ float rowdot(const u32x4 (&k)[3], const u32x4 (&q)[3]) {
  float a0 = 0.f, a1 = 0.f;
#pragma unroll
  for (int j = 0; j < 3; ++j) {
    a0 = dot2(k[j][0], q[j][0], a0); a1 = dot2(k[j][1], q[j][1], a1);
    a0 = dot2(k[j][2], q[j][2], a0); a1 = dot2(k[j][3], q[j][3], a1);
  }
  return a0 + a1;
}
__device__ __forceinline__ float red8(float x) { x += dpp<XOR1>(x); x += dpp<XOR2>(x); x += dpp<XOR7>(x); return x; }

__device__ __forceinline__ u64 tagged_add_slow(u64* w, unsigned tag, u64 inc) {
  for (;;) {
    u64 e = __hip_atomic_load(w, __ATOMIC_RELAXED, __HIP_MEMORY_SCOPE_AGENT);
    while ((unsigned)(e >> 32) != tag &&
           !__hip_atomic_compare_exchange_strong(w, &e, (u64)tag << 32, __ATOMIC_RELAXED, __ATOMIC_RELAXED, __HIP_MEMORY_SCOPE_AGENT)) {}
    const u64 t = __hip_atomic_fetch_add(w, inc, __ATOMIC_RELAXED, __HIP_MEMORY_SCOPE_AGENT);
    if ((unsigned)(t >> 32) == tag) return t;
  }
}

#define SB() do { asm volatile("" ::: "memory"); __builtin_amdgcn_sched_barrier(0); } while (0)
constexpr float LOG2E = 1.4426950408889634f;
constexpr float SCALE = 0.07216878364870322f;
constexpr float C2 = SCALE * LOG2E;

#define STAMP(k)
__device__ __forceinline__ unsigned rotl7(unsigned x) { return (x << 7) | (x >> 25); }

__global__ void __launch_bounds__(64, 2)
attn_tree_decode(const uint16_t* q, const uint16_t* kc, const uint16_t* vc, const uint16_t* kt, const uint16_t* vt,
                 const unsigned* mask, uint16_t* out, unsigned char* ws) {
  using u64x8 = __attribute__((ext_vector_type(8))) u64;
  u64x8 ka;
  asm volatile("s_load_dwordx16 %0, %1, 0x0" : "=s"(ka) : "s"(__builtin_amdgcn_kernarg_segment_ptr()));
  __builtin_amdgcn_sched_barrier(0);
  unsigned codepf;
  {
    u64 pc;
    asm volatile("s_getpc_b64 %0" : "=s"(pc));
    const auto crs = __builtin_amdgcn_make_buffer_rsrc((void*)(__attribute__((address_space(1))) char*)pc, 0, CODEPF, 0x00020000);
    codepf = __builtin_amdgcn_raw_buffer_load_b32(crs, threadIdx.x * 128, 0, 0);
    __builtin_amdgcn_sched_barrier(0);
  }
  __shared__ __attribute__((aligned(16))) f32x4 lds[256 + 64 + 16];
  const unsigned bid = blockIdx.x;
  const unsigned sp = bid % NW, team = (bid / NW + MIX * sp) % BH;
  const int lane = threadIdx.x;
  const int r = lane >> 3, c = lane & 7;
  STAMP(0);

  struct Unit { int t, h, p, n8, tile0, phi; };
  const unsigned k0 = (team * 171u) >> 9, res = team - 3 * k0;
  auto unit = [&](unsigned t, unsigned h, unsigned p) {
    Unit u; u.t = t; u.h = h; u.p = p; u.n8 = p == 1 ? 160 : 176;
    const int lo = p == 0 ? 0 : p == 1 ? 176 : 336;
    unsigned f = (__umul24(p * NT + t * PH3, 43691u) >> 17) + u.n8 - lo; f = min(f, f - u.n8); f = min(f, f - u.n8);
    u.tile0 = h * NT + lo; u.phi = f;
    return u;
  };
  auto tile_off = [](const Unit& u, unsigned pos) { pos += u.phi; return (int)__umul24(u.tile0 + min(pos, pos - u.n8), TB); };
  const Unit u0 = unit(team, k0, res);
  const int voff = r * ROWB + c * 16;

  u32x4 A[UNR][3], B[UNR][3], qf[3];
#define LOADT(R, RS, OFF) do { _Pragma("unroll") for (int u = 0; u < UNR; ++u) _Pragma("unroll") for (int j = 0; j < 3; ++j) \
      R[u][j] = buf_ld128(RS, (OFF) + u * 8 * ROWB + j * 128, 0, AUX_NT); } while (0)
  int o0 = voff + tile_off(u0, sp), o1 = voff + tile_off(u0, 8 + ((sp + 1) & 7));
  asm volatile("" : "+v"(o0), "+v"(o1));
  __builtin_amdgcn_sched_barrier(0);
  asm volatile("s_waitcnt lgkmcnt(0)" : "+s"(ka));
#define GPTR(T, i) ((T*)(__attribute__((address_space(1))) T*)ka[i])
  q = GPTR(const uint16_t, 0); kt = GPTR(const uint16_t, 3); vt = GPTR(const uint16_t, 4);
  mask = GPTR(const unsigned, 5); out = GPTR(uint16_t, 6); ws = GPTR(unsigned char, 7);
  if (__builtin_expect(bid >= BH * NW, 0)) {
    const int h = bid - BH * NW;
    const unsigned* const epoch = reinterpret_cast<const unsigned*>(ws + WS_EPOCH);
    u32x4 tq[3], tk[3], g;
#pragma unroll
    for (int j = 0; j < 3; ++j) {
      tq[j] = *reinterpret_cast<const u32x4*>(q + h * D + j * 64 + c * 8);
      tk[j] = *reinterpret_cast<const u32x4*>(kt + h * D + j * 64 + c * 8);
      g[j] = *reinterpret_cast<const unsigned*>(vt + h * D + j * 64 + (lane & 31) * 2);
    }
    const unsigned tg = __hip_atomic_load(epoch + h * 4 + (r & 3), __ATOMIC_RELAXED, __HIP_MEMORY_SCOPE_AGENT) + TAGSTEP;
    const unsigned mw = mask[0];
    const float mk = __uint_as_float(h >= NH ? (mw & 0xffff0000u) : (mw << 16));
    const float st = (red8(rowdot(tk, tq)) * SCALE + (1.0f - mk) * -1000000000.0f) * LOG2E;
    g[3] = __float_as_uint(st) ^ ((lane & 1) ? rotl7(tg) : tg);
    const auto trs = __builtin_amdgcn_make_buffer_rsrc((void*)(ws + WS_REC + (h * NREC + 3 * NW) * RECB), 0, RECB, 0x00020000);
    __builtin_amdgcn_raw_buffer_store_b128(g, trs, lane * 16, 0, AUX_SC1);
    if (lane == 0) {
      u64* const w = reinterpret_cast<u64*>(ws + WS_TICKET + h * TKB);
      const unsigned tag = __hip_atomic_load(epoch + (128 + h) / 3 * 4, __ATOMIC_RELAXED, __HIP_MEMORY_SCOPE_AGENT) + TAGSTEP;
      u64 e = __hip_atomic_load(w, __ATOMIC_RELAXED, __HIP_MEMORY_SCOPE_AGENT);
      if ((unsigned)(e >> 32) != tag)
        __hip_atomic_compare_exchange_strong(w, &e, (u64)tag << 32, __ATOMIC_RELAXED, __ATOMIC_RELAXED, __HIP_MEMORY_SCOPE_AGENT);
    }
    return;
  }
  const i32x4 krs = {(int)ka[1], (int)(ka[1] >> 32) & 0xffff, KVB, 0x00020000};
  const i32x4 vrs = {(int)ka[2], (int)(ka[2] >> 32) & 0xffff, KVB, 0x00020000};
  LOADT(A, krs, o0);
  SB();
  const auto qrs = __builtin_amdgcn_make_buffer_rsrc((void*)q, 0, BH * ROWB, 0x00020000);
#pragma unroll
  for (int j = 0; j < 3; ++j) qf[j] = __builtin_amdgcn_raw_buffer_load_b128(qrs, u0.h * ROWB + c * 16 + j * 128, 0, 0);
  SB();
  LOADT(B, krs, o1);
  SB();
  const int r1 = u0.n8 / 8, r2 = r1 + (res == 0 ? 20 : 22);
  const bool g1 = lane >= r1, g2 = lane >= r2;
  unsigned x = k0 + (g2 ? SH2 * sp : g1 ? SH1 * sp : 0);
  { const unsigned cnt = res == 0 ? 22 : 21; x = min(x, x - cnt); x = min(x, x - cnt); }
  const unsigned e = (g2 ? 128 : g1 ? 64 : 0) + res, hq = __umul24(e, 171u) >> 9;
  const Unit ul = unit(__umul24(x, 3) + res, x + hq, e - __umul24(hq, 3));
  const int rr = lane - (g2 ? r2 : g1 ? r1 : 0);
  const int tbl = tile_off(ul, rr * 8 + ((sp + rr) & 7));
  const int h1 = __builtin_amdgcn_readlane(ul.h, r1), h2 = __builtin_amdgcn_readlane(ul.h, 63);
  constexpr int half = NS0 / 2;
  const int b1 = r1 / 2, b2 = r2 / 2;
  f32x4* const myfifo = lds + r;
  u32x4* const sq = reinterpret_cast<u32x4*>(lds + 256);
  unsigned* const se = reinterpret_cast<unsigned*>(sq + 64);
  const auto ers = __builtin_amdgcn_make_buffer_rsrc((void*)(ws + WS_EPOCH), 0, BH * 16, 0x00020000);
  const int eoff = (lane < 4 ? u0.h : lane < 8 ? h1 : lane < 12 ? h2 : team) * 16 + (lane & 3) * 4;
  const int qo1 = h1 * ROWB, qo2 = h2 * ROWB - 24 * 16;
  const u32x4 sideq = __builtin_amdgcn_raw_buffer_load_b128(qrs, (lane < 24 ? qo1 : qo2) + lane * 16, 0, 0);
  const unsigned sidee = __builtin_amdgcn_raw_buffer_load_b32(ers, eoff, 0, AUX_SC1);
  float mx = -INFINITY, mxa = -INFINITY, mxb = -INFINITY;
  SB();
  asm volatile("" :: "v"(codepf));
  asm volatile("" : "+v"(qf[0]), "+v"(qf[1]), "+v"(qf[2]));

#pragma nounroll
  for (int n = 1; n <= half; ++n) {
    const int r0 = n < half ? 2 * n : 0;
    int oa = voff + __builtin_amdgcn_readlane(tbl, r0), ob = voff + __builtin_amdgcn_readlane(tbl, r0 + 1);
    i32x4 rs = n < half ? krs : vrs;
    asm volatile("" : "+v"(oa), "+v"(ob));
    asm volatile("" : "+s"(rs));
    SB();
    float s0 = rowdot(A[0], qf), s1 = rowdot(A[1], qf);
    asm volatile("" : "+v"(s0), "+v"(s1));
    SB();
    STAMP(n == 1 ? 6 : n == 2 ? 7 : 64);
    NOPS(DK);
    LOADT(A, rs, oa);
    SB();
    float s2 = rowdot(B[0], qf), s3 = rowdot(B[1], qf);
    asm volatile("" : "+v"(s2), "+v"(s3));
    SB();
    STAMP((n & 1) ? 64 : 7 + (n >> 1));
    NOPS(DK);
    LOADT(B, rs, ob);
    SB();
    const f32x4 s = {red8(s0) * C2, red8(s1) * C2, red8(s2) * C2, red8(s3) * C2};
    mx = fmaxf(fmaxf(mx, s[0]), fmaxf(fmaxf(s[1], s[2]), s[3]));
    myfifo[(n - 1) * 8] = s;
    if (n == b1 || n == b2) {
      if (n == b1) { sq[lane] = sideq; se[lane] = sidee; }
#pragma unroll
      for (int j = 0; j < 3; ++j) qf[j] = sq[(n == b1 ? 0 : 24) + j * 8 + c];
      asm volatile("" : "+v"(qf[0]), "+v"(qf[1]), "+v"(qf[2]));
      mxa = mxb; mxb = mx; mx = -INFINITY;
    }
  }

  const int recv = (ul.h * NREC + ul.p * NW) * RECB;
  const int rec0 = __builtin_amdgcn_readlane(recv, 0), rec1 = __builtin_amdgcn_readlane(recv, r1), rec2 = __builtin_amdgcn_readlane(recv, 63);
  Unit u2;
  u2.n8 = __builtin_amdgcn_readlane(ul.n8, 63); u2.tile0 = __builtin_amdgcn_readlane(ul.tile0, 63); u2.phi = __builtin_amdgcn_readlane(ul.phi, 63);
  u64* const word = reinterpret_cast<u64*>(ws + WS_TICKET + __builtin_amdgcn_readlane(ul.t, 63) * TKB);
  const auto wrs = __builtin_amdgcn_make_buffer_rsrc((void*)word, 0, 8, 0x00020000);
  const auto prs = __builtin_amdgcn_make_buffer_rsrc((void*)(ws + WS_REC), 0, BH * NREC * RECB, 0x00020000);
  u32x4 sel;
#pragma unroll
  for (int k = 0; k < 4; ++k) sel[k] = (k == (c >> 1)) ? ((c & 1) ? 0xffff0000u : 0x0000ffffu) : 0u;
  const int gpos = (lane & 8) ? OOB : (4 * c + ((lane >> 3) & 2) + (lane >> 5)) * 16 + sp * RECB;
  int lane64 = lane * 64;
  asm volatile("" : "+v"(lane64));
#define MAKE_P(P, S) do { const float p_ = __builtin_amdgcn_exp2f((S) - m); l += p_; const unsigned pb_ = pk_bf16(p_, p_); \
    P = u32x4{pb_ & sel[0], pb_ & sel[1], pb_ & sel[2], pb_ & sel[3]}; asm volatile("" : "+v"(P)); } while (0)
#define PV(P, R) do { _Pragma("unroll") for (int j = 0; j < 3; ++j) acc[j] = mfma(P, R[j], acc[j]); } while (0)

  u32x4 ga = {0u, 0u, 0u, 0u}, gb = ga;
  long long tkt = 0;
  int n = 1;
#pragma nounroll
  for (int g = 0; g < 3; ++g) {
    float m = fmaxf(mxa, dpp<XOR8>(mxa)), l = 0.f;
    m = x16_max(m); m = x32_max(m);
    mxa = mxb; mxb = mx;
    f32x4 acc[3];
#pragma unroll
    for (int j = 0; j < 3; ++j) acc[j] = f32x4{0.f, 0.f, 0.f, 0.f};
    const int nend = g == 0 ? b1 : g == 1 ? b2 : half;
#pragma nounroll
    do {
      const f32x4 s = myfifo[(n - 1) * 8];
      int oa = voff + __builtin_amdgcn_readlane(tbl, 2 * n), ob = voff + __builtin_amdgcn_readlane(tbl, n < half ? 2 * n + 1 : 2 * n);
      i32x4 rs = n < half ? vrs : krs;
      if (n == half) tkt = buf_add64(1, wrs, lane64, 0, 0);
      asm volatile("" : "+v"(oa), "+v"(ob));
      asm volatile("" : "+s"(rs));
      u32x4 P0, P1, P2, P3;
      MAKE_P(P0, s[0]); MAKE_P(P1, s[1]); MAKE_P(P2, s[2]); MAKE_P(P3, s[3]);
      SB();
      PV(P0, A[0]); PV(P1, A[1]);
      SB();
      NOPS(DV);
      LOADT(A, rs, oa);
      SB();
      PV(P2, B[0]); PV(P3, B[1]);
      SB();
      STAMP(((n + half) & 1) ? 64 : 7 + ((n + half) >> 1));
      NOPS(DV);
      LOADT(B, vrs, ob);
      SB();
    } while (++n <= nend);
    if (g == 2) {
      const int runs = lane + (G1S - 1) * min(lane, G1N) + (G2S - 1) * min(max(lane - G1N, 0), G2N);
      const unsigned tag = __builtin_amdgcn_readfirstlane(se[8]) + TAGSTEP;
      int cur = 0, rem = 0;
      bool more;
#pragma nounroll
      do {
        if (rem == 0) {
          unsigned tlo = __builtin_amdgcn_readfirstlane((unsigned)tkt);
          const unsigned thi = __builtin_amdgcn_readfirstlane((unsigned)((u64)tkt >> 32));
          if (__builtin_expect(thi != tag, 0)) {
            u64 t = 0;
            if (lane == 0) t = tagged_add_slow(word, tag, 1);
            tlo = __builtin_amdgcn_readfirstlane((unsigned)t);
          }
          const int t = min((int)tlo, 62);
          cur = __builtin_amdgcn_readlane(runs, t);
          rem = __builtin_amdgcn_readlane(runs, t + 1) - cur;
        }
        more = cur < POOL;
        int o = voff + (more ? tile_off(u2, u2.n8 - POOL + cur) : OOB);
        ++cur; --rem;
        asm volatile("" : "+v"(o));
        tkt = buf_add64(1, wrs, (rem == 0 && more) ? lane64 : 64, 0, 0);
        __builtin_amdgcn_raw_buffer_store_b128(ga, prs, gpos + (more ? OOB : rec0), 0, AUX_SC1);
        __builtin_amdgcn_raw_buffer_store_b128(gb, prs, gpos + (more ? OOB : rec1), 0, AUX_SC1);
        SB();
        float s0 = rowdot(A[0], qf), s1 = rowdot(A[1], qf);
        asm volatile("" : "+v"(s0), "+v"(s1));
        SB();
        LOADT(A, krs, o);
        SB();
        s0 = red8(s0) * C2; s1 = red8(s1) * C2;
        const float x = fmaxf(s0, s1);
        if (__builtin_amdgcn_ballot_w64(x > m) != 0) {
          float mn = fmaxf(x, dpp<XOR8>(x)); mn = x16_max(mn); mn = x32_max(mn); mn = fmaxf(mn, m);
          const float alpha = __builtin_amdgcn_exp2f(m - mn);
          m = mn; l *= alpha;
#pragma unroll
          for (int j = 0; j < 3; ++j) acc[j] *= alpha;
        }
        u32x4 P0, P1;
        MAKE_P(P0, s0); MAKE_P(P1, s1);
        asm volatile("" : "+v"(o));
        SB();
        PV(P0, B[0]); PV(P1, B[1]);
        SB();
        LOADT(B, vrs, o);
        SB();
      } while (more);
      STAMP(1);
    }
    const unsigned tg = se[4 * g + (c >> 1)] + TAGSTEP;
    l += dpp<XOR8>(l); l = x16_sum(l); l = x32_sum(l);
    const float inv = __builtin_amdgcn_rcpf(l);
    ga = gb;
#pragma unroll
    for (int j = 0; j < 3; ++j) {
      const auto w0 = __builtin_amdgcn_permlane32_swap(__float_as_uint(acc[j][0]), __float_as_uint(acc[j][2]), false, false);
      const auto w1 = __builtin_amdgcn_permlane32_swap(__float_as_uint(acc[j][1]), __float_as_uint(acc[j][3]), false, false);
      const float t0 = __uint_as_float(w0[0]) + dpp<XOR8>(__uint_as_float(w0[1]));
      const float t1 = __uint_as_float(w1[0]) + dpp<XOR8>(__uint_as_float(w1[1]));
      gb[j] = pk_bf16(t0 * inv, t1 * inv);
    }
    gb[3] = __float_as_uint(m + __builtin_amdgcn_logf(l)) ^ (lane >= 32 ? rotl7(tg) : tg);
  }
  __builtin_amdgcn_raw_buffer_store_b128(gb, prs, gpos + rec2, 0, AUX_SC1);
  STAMP(2);

  const int ln = sp & 3;
  if ((sp >> 2) == (team & 1)) {
    asm volatile("" ::: "memory");
    const unsigned tg = se[12 + ln] + TAGSTEP, key = tg ^ rotl7(tg), mine = (c & 1) ? rotl7(tg) : tg;
    const int ro = (team * NREC + r) * RECB + ln * 128 + c * 16;
    u32x4 a[4];
    bool miss[4] = {true, true, true, r == 0};
    a[3] = u32x4{0u, 0u, 0u, 0u};
    const u64 t0 = __builtin_amdgcn_s_memrealtime();
    for (;;) {
#pragma unroll
      for (int k = 0; k < 4; ++k)
        if (miss[k]) a[k] = __builtin_amdgcn_raw_buffer_load_b128(prs, ro, k * NW * RECB, AUX_SC1);
#pragma unroll
      for (int k = 0; k < 4; ++k)
        miss[k] = (a[k][3] ^ (unsigned)__builtin_amdgcn_mov_dpp((int)a[k][3], XOR1, 0xf, 0xf, true)) != key && (k < 3 || r == 0);
      if (__builtin_amdgcn_ballot_w64(miss[0] || miss[1] || miss[2] || miss[3]) == 0 ||
          __builtin_amdgcn_s_memrealtime() - t0 > 20000000ull) break;
      if (BACKOFF) __builtin_amdgcn_s_sleep(BACKOFF);
      asm volatile("" ::: "memory");
    }
    STAMP(3);
    float ls[4];
#pragma unroll
    for (int k = 0; k < 4; ++k) ls[k] = __uint_as_float(a[k][3] ^ mine);
    ls[3] = r == 0 ? ls[3] : -INFINITY;
    float M = fmaxf(fmaxf(ls[0], ls[1]), fmaxf(ls[2], ls[3]));
    M = fmaxf(M, dpp<XOR8>(M)); M = x16_max(M); M = x32_max(M);
    float W = 0.f;
    f32x2 o[3] = {{0.f, 0.f}, {0.f, 0.f}, {0.f, 0.f}};
#pragma unroll
    for (int k = 0; k < 4; ++k) {
      const float w = __builtin_amdgcn_exp2f(ls[k] - M);
      W += w;
#pragma unroll
      for (int j = 0; j < 3; ++j) o[j] += w * f32x2{__uint_as_float(a[k][j] << 16), __uint_as_float(a[k][j] & 0xffff0000u)};
    }
    float x0, x1, y0, y1;
    {
      const auto p0 = __builtin_amdgcn_permlane32_swap(__float_as_uint(o[0][0]), __float_as_uint(o[2][0]), false, false);
      const auto p1 = __builtin_amdgcn_permlane32_swap(__float_as_uint(o[0][1]), __float_as_uint(o[2][1]), false, false);
      const auto p2 = __builtin_amdgcn_permlane32_swap(__float_as_uint(o[1][0]), __float_as_uint(W), false, false);
      const auto p3 = __builtin_amdgcn_permlane32_swap(__float_as_uint(o[1][1]), __float_as_uint(W), false, false);
      x0 = __uint_as_float(p0[0]) + __uint_as_float(p0[1]); x1 = __uint_as_float(p1[0]) + __uint_as_float(p1[1]);
      y0 = __uint_as_float(p2[0]) + __uint_as_float(p2[1]); y1 = __uint_as_float(p3[0]) + __uint_as_float(p3[1]);
    }
    const auto u0 = __builtin_amdgcn_permlane16_swap(__float_as_uint(x0), __float_as_uint(y0), false, false);
    const auto u1 = __builtin_amdgcn_permlane16_swap(__float_as_uint(x1), __float_as_uint(y1), false, false);
    float z0 = __uint_as_float(u0[0]) + __uint_as_float(u0[1]), z1 = __uint_as_float(u1[0]) + __uint_as_float(u1[1]);
    z0 += dpp<XOR8>(z0); z1 += dpp<XOR8>(z1);
    const float inv = __builtin_amdgcn_rcpf(__builtin_bit_cast(float, __builtin_amdgcn_readlane(__builtin_bit_cast(int, z0), 48)));
    const auto ors = __builtin_amdgcn_make_buffer_rsrc((void*)(out + team * D), 0, D * 2, 0x00020000);
    __builtin_amdgcn_raw_buffer_store_b32(pk_bf16(z0 * inv, z1 * inv), ors, (lane & 8) ? OOB : (lane >> 4) * 128 + ln * 32 + c * 4, 0, 0);
    __builtin_amdgcn_raw_buffer_store_b32(tg, ers, lane == 0 ? team * 16 + ln * 4 : OOB, 0, AUX_SC1);
  }
  STAMP(4);
}

extern "C" void kernel_launch(void* const* d_in, const int* in_sizes, int n_in, void* d_out, int out_size, void* d_ws,
                              size_t ws_size, hipStream_t stream) {
  attn_tree_decode<<<BH * NW + BH, 64, 0, stream>>>((const uint16_t*)d_in[0], (const uint16_t*)d_in[1], (const uint16_t*)d_in[2],
                                                (const uint16_t*)d_in[3], (const uint16_t*)d_in[4], (const unsigned*)d_in[5],
                                                (uint16_t*)d_out, (unsigned char*)d_ws);
}
```
